```python
import torch

def _softmax(x, axis=-1):
    return torch.softmax(x, dim=axis)

def _gelu(x):
    return torch.nn.functional.gelu(x, approximate='tanh')

def _silu(x):
    return torch.nn.functional.silu(x)

def _sigmoid(x):
    return torch.sigmoid(x)

def _layernorm(x, axis=-1, eps=1e-05):
    m = x.mean(dim=axis, keepdim=True)
    v = x.var(dim=axis, keepdim=True, unbiased=False)
    return (x - m) / torch.sqrt(v + eps)

def _scatter_add(tgt, idx, src, axis=0):
    return tgt.clone().index_add_(axis, idx, src)

def _segment_sum(src, seg, n):
    out = torch.zeros((n,) + src.shape[1:], dtype=src.dtype)
    return out.index_add_(0, seg, src)

def _pad(x, pw):
    flat = tuple((v for pair in reversed(pw) for v in pair))
    return torch.nn.functional.pad(x, flat)
kernel_name = 'standalone__ragged_sequences_varlen_flash_attn__ragged'

def setup_inputs():

    _w = lambda a: (lambda t: t.to(torch.bfloat16) if t.is_floating_point() else t)(torch.as_tensor(a))
    cu = torch.tensor([0, 485, 1609, 2680, 3050, 3769, 5493, 6501, 7684], dtype=torch.int64)
    T = int(cu[-1])
    Hq, Hkv, D = (16, 8, 128)
    assert Hq % Hkv == 0
    return {'q': _w((lambda *s: torch.randn(*s, dtype=torch.bfloat16) * 0.8)(T, Hq, D)), 'k': _w((lambda *s: torch.randn(*s, dtype=torch.bfloat16) * 0.8)(T, Hkv, D)), 'v': _w((lambda *s: torch.randn(*s, dtype=torch.bfloat16) * 0.05)(T, Hkv, D)), 'cu': _w(cu)}

def reference(q, k, v, cu):
    T, Hq, D = q.shape
    Hkv = k.shape[1]
    rep = Hq // Hkv
    scale = D ** (-0.5)
    outs = []
    for i in range(len(cu) - 1):
        s, e = (int(cu[i]), int(cu[i + 1]))
        L = e - s
        qi = q[s:e].clone()
        ki = (lambda x, n, axis: torch.repeat_interleave(x, n, dim=axis))(k[s:e], rep, axis=1)
        vi = (lambda x, n, axis: torch.repeat_interleave(x, n, dim=axis))(v[s:e], rep, axis=1)
        logits = torch.einsum('qhd,khd->hqk', qi, ki) * scale
        ar = torch.arange(L)
        neg = (lambda x, dt: x.to(dt))(ar[None, :] > ar[:, None], logits.dtype) * -1000000000.0
        p = _softmax(logits + neg[None, :, :], axis=-1)
        outs.append(torch.einsum('hqk,khd->qhd', p, vi))
    return torch.concatenate(outs, axis=0)
```

```cpp
#include <hip/hip_runtime.h>
#include <hip/hip_bf16.h>
#include <cstdint>

typedef short bf16x8 __attribute__((ext_vector_type(8)));
typedef short s16x4 __attribute__((ext_vector_type(4)));
typedef float f32x16 __attribute__((ext_vector_type(16)));
typedef unsigned u32x4 __attribute__((ext_vector_type(4)));

constexpr int D = 128, HQ = 16, HKV = 8;
constexpr int QSTR = HQ * D;
constexpr int KSTR = HKV * D;
constexpr int QB = 128;
constexpr int KVBLK = 64;
constexpr int SHM_T = KVBLK * D * 2;
constexpr int OROW = 272;
constexpr int LDS_KV = 4 * SHM_T;
constexpr int LDS_BYTES = LDS_KV + 8 * 32 * OROW;
constexpr float CEXP = 0.08838834764831845f * 1.4426950408889634f;

#define SBAR() __builtin_amdgcn_sched_barrier(0)

__device__ __forceinline__ unsigned cvtpk(float lo, float hi) {
    unsigned r; asm volatile("v_cvt_pk_bf16_f32 %0, %1, %2" : "=v"(r) : "v"(lo), "v"(hi)); return r;
}
__device__ __forceinline__ int crow(int r, int hi) { return (r & 3) + 8 * (r >> 2) + 4 * hi; }

__device__ __forceinline__ int v_st(int k, int c) { return ((k >> 3) * 4 + (c >> 5)) * 512 + ((k & 7) * 32 + (c & 31)) * 2; }
__device__ __forceinline__ int v_rd_base(int lane) { return ((lane & 3) << 3) | (((lane >> 2) & 3) << 6) | (((lane >> 4) & 1) << 5) | (((lane >> 5) & 1) << 8); }

template <int KB>
__device__ __forceinline__ void qkt(f32x16& p0, f32x16& p1, const char* K_lds, const int* kad, const bf16x8* qr) {
    p0 = f32x16{}; p1 = f32x16{};
#pragma unroll
    for (int d0 = 0; d0 < 8; ++d0) {
        const char* a = K_lds + KB * SHM_T + kad[d0 & 3] + (d0 >> 2) * 128;
        bf16x8 b0 = *reinterpret_cast<const bf16x8*>(a);
        bf16x8 b1 = *reinterpret_cast<const bf16x8*>(a + 32 * 256);
        p0 = __builtin_amdgcn_mfma_f32_32x32x16_bf16(b0, qr[d0], p0, 0, 0, 0);
        p1 = __builtin_amdgcn_mfma_f32_32x32x16_bf16(b1, qr[d0], p1, 0, 0, 0);
    }
}

template <int VB>
__device__ __forceinline__ void pv_tile(f32x16* o, int vb0, bf16x8 pa0, bf16x8 pa1, bf16x8 pa2, bf16x8 pa3) {
#define TRRD(dst, off) asm volatile("ds_read_b64_tr_b16 %0, %1 offset:%2" : "=&v"(dst) : "v"(vb0), "i"(off) : "memory")
#define PV_D0(d0) do { s16x4 l0, l1, l2, l3, h0, h1, h2, h3; constexpr int b_ = VB * SHM_T + d0 * 512; \
        TRRD(l0, b_); TRRD(h0, b_ + 2048); TRRD(l1, b_ + 4096); TRRD(h1, b_ + 6144); TRRD(l2, b_ + 8192); TRRD(h2, b_ + 10240); TRRD(l3, b_ + 12288); TRRD(h3, b_ + 14336); \
        asm volatile("s_waitcnt lgkmcnt(0)" ::: "memory"); SBAR(); \
        o[d0] = __builtin_amdgcn_mfma_f32_32x32x16_bf16((bf16x8){l0[0], l0[1], l0[2], l0[3], h0[0], h0[1], h0[2], h0[3]}, pa0, o[d0], 0, 0, 0); \
        o[d0] = __builtin_amdgcn_mfma_f32_32x32x16_bf16((bf16x8){l1[0], l1[1], l1[2], l1[3], h1[0], h1[1], h1[2], h1[3]}, pa1, o[d0], 0, 0, 0); \
        o[d0] = __builtin_amdgcn_mfma_f32_32x32x16_bf16((bf16x8){l2[0], l2[1], l2[2], l2[3], h2[0], h2[1], h2[2], h2[3]}, pa2, o[d0], 0, 0, 0); \
        o[d0] = __builtin_amdgcn_mfma_f32_32x32x16_bf16((bf16x8){l3[0], l3[1], l3[2], l3[3], h3[0], h3[1], h3[2], h3[3]}, pa3, o[d0], 0, 0, 0); } while (0)
    PV_D0(0); PV_D0(1); PV_D0(2); PV_D0(3);
#undef PV_D0
#undef TRRD
}

__device__ __forceinline__ void mask_tile(f32x16& p0, f32x16& p1, int dq) {
    const float NEG = -__builtin_inff();
#pragma unroll
    for (int r = 0; r < 16; ++r) {
        const int c = (r & 3) + 8 * (r >> 2);
        if (c > dq) p0[r] = NEG;
        if (c + 32 > dq) p1[r] = NEG;
    }
}

__device__ __forceinline__ void partialSM(f32x16& p0, f32x16& p1, float& m_reg, float& alpha) {
    float pmax = p0[0];
#pragma unroll
    for (int r = 1; r < 16; ++r) pmax = fmaxf(pmax, p0[r]);
#pragma unroll
    for (int r = 0; r < 16; ++r) pmax = fmaxf(pmax, p1[r]);
    { auto rr = __builtin_amdgcn_permlane32_swap(__float_as_uint(pmax), __float_as_uint(pmax), false, false);
      pmax = fmaxf(__uint_as_float(rr[0]), __uint_as_float(rr[1])); }
    const float mn = fmaxf(m_reg, pmax);
    alpha = __builtin_amdgcn_exp2f((m_reg - mn) * CEXP);
    m_reg = mn;
    const float mnL = -mn * CEXP;
#pragma unroll
    for (int r = 0; r < 16; ++r) p0[r] = fmaf(p0[r], CEXP, mnL);
#pragma unroll
    for (int r = 0; r < 16; ++r) p1[r] = fmaf(p1[r], CEXP, mnL);
#pragma unroll
    for (int r = 0; r < 16; ++r) p0[r] = __builtin_amdgcn_exp2f(p0[r]);
}
__device__ __forceinline__ void finishSM(f32x16& p0, f32x16& p1, float alpha, float& l_reg, bf16x8& pa0, bf16x8& pa1, bf16x8& pa2, bf16x8& pa3) {
#pragma unroll
    for (int r = 0; r < 16; ++r) p1[r] = __builtin_amdgcn_exp2f(p1[r]);
    float ps = 0;
#pragma unroll
    for (int r = 0; r < 16; ++r) ps += p0[r];
#pragma unroll
    for (int r = 0; r < 16; ++r) ps += p1[r];
    l_reg = l_reg * alpha + ps;
#define PK8(P, B_, OUT) do { u32x4 w = {cvtpk(P[B_+0], P[B_+1]), cvtpk(P[B_+2], P[B_+3]), cvtpk(P[B_+4], P[B_+5]), cvtpk(P[B_+6], P[B_+7])}; \
        OUT = *reinterpret_cast<bf16x8*>(&w); } while (0)
    PK8(p0, 0, pa0); PK8(p0, 8, pa1); PK8(p1, 0, pa2); PK8(p1, 8, pa3);
#undef PK8
}

__device__ __forceinline__ bool decode_item(const int* __restrict__ cu, int nseq, int rank, int& s_out, int& b_out) {
    const int lane = threadIdx.x & 63;
    if (nseq <= 64) {
        int nb = 0;
        if (lane < nseq) { const int L = cu[lane + 1] - cu[lane]; nb = (L + QB - 1) / QB; }
        int maxnb = nb;
#pragma unroll
        for (int o = 32; o >= 1; o >>= 1) maxnb = max(maxnb, __shfl_xor(maxnb, o));
        for (int bb = maxnb - 1; bb >= 0; --bb) {
            unsigned long long m = __ballot(nb > bb);
            const int c = __popcll(m);
            if (rank < c) {
                for (int i = 0; i < rank; ++i) m &= m - 1;
                s_out = __ffsll((long long)m) - 1; b_out = bb; return true;
            }
            rank -= c;
        }
        return false;
    } else {
        int maxnb = 0;
        for (int s = 0; s < nseq; ++s) { const int L = cu[s + 1] - cu[s]; maxnb = max(maxnb, (L + QB - 1) / QB); }
        for (int bb = maxnb - 1; bb >= 0; --bb)
            for (int s = 0; s < nseq; ++s) {
                const int L = cu[s + 1] - cu[s];
                if ((L + QB - 1) / QB > bb) { if (rank == 0) { s_out = s; b_out = bb; return true; } --rank; }
            }
        return false;
    }
}

__global__ __launch_bounds__(512, 2)
void ragged_attn_kernel(const short* __restrict__ Qg, const short* __restrict__ Kg, const short* __restrict__ Vg,
                        const int* __restrict__ cu, short* __restrict__ Og, int nseq) {
    __shared__ __attribute__((aligned(16))) char lds[LDS_BYTES];
    const int tid = threadIdx.x;
    const int wid = __builtin_amdgcn_readfirstlane(tid >> 6), lane = tid & 63, r32 = lane & 31, hi = lane >> 5;
    const int g = blockIdx.x & 7;
    int s = 0, b = 0;
    if (!decode_item(cu, nseq, blockIdx.x >> 3, s, b)) return;
    s = __builtin_amdgcn_readfirstlane(s); b = __builtin_amdgcn_readfirstlane(b);
    const int start = cu[s], L = cu[s + 1] - start;
    const int NKT = (L + KVBLK - 1) / KVBLK;
    const int NT = min(2 * b + 2, NKT);
    const int head = 2 * g + (wid >> 2);
    const int qrow0 = b * QB + (wid & 3) * 32;
    const int qrow = qrow0 + r32;

    char* K_lds = lds; char* V_lds = lds + 2 * SHM_T;
    bf16x8 qr[8];
    {
        const int qc = min(qrow, L - 1);
        const short* qp = Qg + ((size_t)(start + qc) * HQ + head) * D + hi * 8;
#pragma unroll
        for (int d0 = 0; d0 < 8; ++d0) qr[d0] = *reinterpret_cast<const bf16x8*>(qp + d0 * 16);
    }
    const int sr = tid >> 4, c8 = tid & 15;
    const int kws = sr * 256 + ((c8 ^ (sr & 7)) << 4);
    const int vst0 = v_st(sr, c8 * 8), vst1 = v_st(32 + sr, c8 * 8);
    const __amdgpu_buffer_rsrc_t rsK = __builtin_amdgcn_make_buffer_rsrc((void*)(Kg + ((size_t)start * HKV + g) * D), (short)0, (L * HKV - g) * D * 2, 0x00020000);
    const __amdgpu_buffer_rsrc_t rsV = __builtin_amdgcn_make_buffer_rsrc((void*)(Vg + ((size_t)start * HKV + g) * D), (short)0, (L * HKV - g) * D * 2, 0x00020000);
    const int voff = (sr * KSTR + c8 * 8) * 2;
    bf16x8 st_k0, st_k1, st_v0, st_v1;
#define BL(rs, so) __builtin_bit_cast(bf16x8, __builtin_amdgcn_raw_buffer_load_b128(rs, voff, so, 0))
#define SLOAD(t) do { const int so_ = (t) * KVBLK * KSTR * 2; \
        st_k0 = BL(rsK, so_); st_k1 = BL(rsK, so_ + 32 * KSTR * 2); st_v0 = BL(rsV, so_); st_v1 = BL(rsV, so_ + 32 * KSTR * 2); } while (0)
#define SWRITE(bf) do { *(bf16x8*)(K_lds + (bf) * SHM_T + kws) = st_k0; *(bf16x8*)(K_lds + (bf) * SHM_T + kws + 32 * 256) = st_k1; \
        *(bf16x8*)(V_lds + (bf) * SHM_T + vst0) = st_v0; *(bf16x8*)(V_lds + (bf) * SHM_T + vst1) = st_v1; } while (0)
    int kad[4];
#pragma unroll
    for (int d0 = 0; d0 < 4; ++d0) kad[d0] = r32 * 256 + (((2 * d0 + hi) ^ (r32 & 7)) << 4);
    const int vb0 = (int)(uintptr_t)V_lds + v_rd_base(lane);

    float m_reg = -1e30f, l_reg = 0.f;
    f32x16 o[4] = {};
    f32x16 pA0, pA1, pB0, pB1; float alA, alB; bf16x8 pa0, pa1, pa2, pa3;
    const int qm = qrow - 4 * hi;
#define MASKT(P0_, P1_, t) do { if ((t) * KVBLK + KVBLK - 1 > qrow0) mask_tile(P0_, P1_, qm - (t) * KVBLK); } while (0)
#define RESC(a) do { if (__any((a) < 1.f)) { for (int d_ = 0; d_ < 4; ++d_) for (int r = 0; r < 16; ++r) o[d_][r] *= (a); } } while (0)

    SLOAD(0); SWRITE(0);
    if (NT > 1) SLOAD(1);
    __syncthreads();
    SBAR(); qkt<0>(pA0, pA1, K_lds, kad, qr);
    MASKT(pA0, pA1, 0); partialSM(pA0, pA1, m_reg, alA);
    if (NT > 1) { SWRITE(1); }
    __syncthreads();
#define HALF_STEP(PX0, PX1, alX, PY0, PY1, alY, t, KB, VB, SB) do { \
        SBAR(); qkt<KB>(PX0, PX1, K_lds, kad, qr); \
        finishSM(PY0, PY1, alY, l_reg, pa0, pa1, pa2, pa3); SBAR(); \
        if ((t) + 1 < NT) { SLOAD((t) + 1); SBAR(); } \
        pv_tile<VB>(o, vb0, pa0, pa1, pa2, pa3); MASKT(PX0, PX1, (t)); partialSM(PX0, PX1, m_reg, alX); \
        __syncthreads(); \
        if ((t) + 1 < NT) { SWRITE(SB); } \
        RESC(alX); __syncthreads(); } while (0)
    for (int t = 1; t + 1 < NT; t += 2) {
        HALF_STEP(pB0, pB1, alB, pA0, pA1, alA, t, 1, 0, 0);
        HALF_STEP(pA0, pA1, alA, pB0, pB1, alB, t + 1, 0, 1, 1);
    }
    const bool even = (NT & 1) == 0;
    if (even) { SBAR(); qkt<1>(pB0, pB1, K_lds, kad, qr); SBAR(); }
    finishSM(pA0, pA1, alA, l_reg, pa0, pa1, pa2, pa3); SBAR();
    pv_tile<0>(o, vb0, pa0, pa1, pa2, pa3);
    if (even) {
        MASKT(pB0, pB1, NT - 1); partialSM(pB0, pB1, m_reg, alB); RESC(alB);
        finishSM(pB0, pB1, alB, l_reg, pa0, pa1, pa2, pa3); SBAR(); pv_tile<1>(o, vb0, pa0, pa1, pa2, pa3);
    }
    int lane2; asm volatile("v_mbcnt_lo_u32_b32 %0, -1, 0\n\tv_mbcnt_hi_u32_b32 %0, -1, %0" : "=v"(lane2));
    const float lt = l_reg + __shfl_xor(l_reg, 32);
    const float inv = 1.0f / lt;
    char* ost = lds + LDS_KV + wid * (32 * OROW);
#pragma unroll
    for (int d0 = 0; d0 < 4; ++d0)
#pragma unroll
        for (int a = 0; a < 4; ++a) {
            uint2 w;
            w.x = cvtpk(o[d0][4 * a + 0] * inv, o[d0][4 * a + 1] * inv);
            w.y = cvtpk(o[d0][4 * a + 2] * inv, o[d0][4 * a + 3] * inv);
            *(uint2*)(ost + (lane2 & 31) * OROW + (32 * d0 + 8 * a + 4 * (lane2 >> 5)) * 2) = w;
        }
    asm volatile("s_waitcnt lgkmcnt(0)" ::: "memory");
    __builtin_amdgcn_wave_barrier();
#pragma unroll
    for (int it = 0; it < 8; ++it) {
        const int rr = 4 * it + (lane2 >> 4), ch = lane2 & 15;
        const u32x4 w = *(const u32x4*)(ost + rr * OROW + ch * 16);
        const int orow = qrow0 + rr;
        if (orow < L) *(u32x4*)(Og + ((size_t)(start + orow) * HQ + head) * D + ch * 8) = w;
    }
}

extern "C" void kernel_launch(void* const* d_in, const int* in_sizes, int n_in,
                              void* d_out, int out_size, void* d_ws, size_t ws_size,
                              hipStream_t stream) {
    (void)n_in; (void)out_size; (void)d_ws; (void)ws_size;
    const short* q = (const short*)d_in[0];
    const short* k = (const short*)d_in[1];
    const short* v = (const short*)d_in[2];
    const int* cu = (const int*)d_in[3];
    short* out = (short*)d_out;
    const int T = in_sizes[0] / (HQ * D);
    const int nseq = in_sizes[3] - 1;
    const int maxpairs = (T + QB - 1) / QB + nseq;
    ragged_attn_kernel<<<dim3(8 * maxpairs), dim3(512), 0, stream>>>(q, k, v, cu, out, nseq);
}
```

```cpp
#include <hip/hip_runtime.h>
#include <hip/hip_bf16.h>
#include <cstdint>

typedef short bf16x8 __attribute__((ext_vector_type(8)));
typedef short s16x4 __attribute__((ext_vector_type(4)));
typedef float f32x16 __attribute__((ext_vector_type(16)));
typedef unsigned u32x4 __attribute__((ext_vector_type(4)));
typedef __attribute__((address_space(3))) s16x4* lds_s16x4_p;

constexpr int D = 128, HQ = 16, HKV = 8;
constexpr int KSTR = HKV * D;
constexpr int QB = 128;
constexpr int KVBLK = 64;
constexpr int SHM_T = KVBLK * D * 2;
constexpr int OROW = 272;
constexpr int LDS_KV = 4 * SHM_T;
constexpr int LDS_BYTES = LDS_KV + 8 * 32 * OROW;
constexpr float CEXP = 0.08838834764831845f * 1.4426950408889634f;

__device__ __forceinline__ unsigned cvtpk(float lo, float hi) {
    unsigned r; asm volatile("v_cvt_pk_bf16_f32 %0, %1, %2" : "=v"(r) : "v"(lo), "v"(hi)); return r;
}
__device__ __forceinline__ int v_st(int k, int c) { return ((k >> 3) * 4 + (c >> 5)) * 512 + ((k & 7) * 32 + (c & 31)) * 2; }
__device__ __forceinline__ int v_rd_base(int lane) { return ((lane & 3) << 3) | (((lane >> 2) & 3) << 6) | (((lane >> 4) & 1) << 5) | (((lane >> 5) & 1) << 8); }

__device__ __forceinline__ bool decode_item(const int* __restrict__ cu, int nseq, int rank, int& s_out, int& b_out) {
    const int lane = threadIdx.x & 63;
    if (nseq <= 64) {
        int nb = 0;
        if (lane < nseq) { const int L = cu[lane + 1] - cu[lane]; nb = (L + QB - 1) / QB; }
        int maxnb = nb;
#pragma unroll
        for (int o = 32; o >= 1; o >>= 1) maxnb = max(maxnb, __shfl_xor(maxnb, o));
        for (int bb = maxnb - 1; bb >= 0; --bb) {
            unsigned long long m = __ballot(nb > bb);
            const int c = __popcll(m);
            if (rank < c) {
                for (int i = 0; i < rank; ++i) m &= m - 1;
                s_out = __ffsll((long long)m) - 1; b_out = bb; return true;
            }
            rank -= c;
        }
        return false;
    } else {
        int maxnb = 0;
        for (int s = 0; s < nseq; ++s) { const int L = cu[s + 1] - cu[s]; maxnb = max(maxnb, (L + QB - 1) / QB); }
        for (int bb = maxnb - 1; bb >= 0; --bb)
            for (int s = 0; s < nseq; ++s) {
                const int L = cu[s + 1] - cu[s];
                if ((L + QB - 1) / QB > bb) { if (rank == 0) { s_out = s; b_out = bb; return true; } --rank; }
            }
        return false;
    }
}

#define MFMA(a, b, c) __builtin_amdgcn_mfma_f32_32x32x16_bf16(a, b, c, 0, 0, 0)

__device__ __forceinline__ void qk_phase(f32x16& p0, f32x16& p1, const char* kbase, const int* kad, const bf16x8* qr) {
    p0 = f32x16{}; p1 = f32x16{};
#pragma unroll
    for (int d0 = 0; d0 < 8; ++d0) {
        const char* a = kbase + kad[d0 & 3] + (d0 >> 2) * 128;
        const bf16x8 b0 = *reinterpret_cast<const bf16x8*>(a);
        const bf16x8 b1 = *reinterpret_cast<const bf16x8*>(a + 32 * 256);
        p0 = MFMA(b0, qr[d0], p0);
        p1 = MFMA(b1, qr[d0], p1);
    }
}
__device__ __forceinline__ s16x4 trrd(const char* p) { return __builtin_amdgcn_ds_read_tr16_b64_v4i16((lds_s16x4_p)p); }
__device__ __forceinline__ void pv_phase(f32x16* o, const char* vrd, const bf16x8* pa) {
#pragma unroll
    for (int d0 = 0; d0 < 4; ++d0)
#pragma unroll
        for (int s = 0; s < 4; ++s) {
            const s16x4 lo = trrd(vrd + d0 * 512 + s * 4096), hi = trrd(vrd + d0 * 512 + s * 4096 + 2048);
            const bf16x8 a = __builtin_shufflevector(lo, hi, 0, 1, 2, 3, 4, 5, 6, 7);
            o[d0] = MFMA(a, pa[s], o[d0]);
        }
}

__global__ __launch_bounds__(512, 2)
void ragged_attn_kernel(const short* __restrict__ Qg, const short* __restrict__ Kg, const short* __restrict__ Vg,
                        const int* __restrict__ cu, short* __restrict__ Og, int nseq) {
    __shared__ __attribute__((aligned(16))) char lds[LDS_BYTES];
    const int tid = threadIdx.x;
    const int wid = __builtin_amdgcn_readfirstlane(tid >> 6), lane = tid & 63, r32 = lane & 31, hi = lane >> 5;
    const int g = blockIdx.x & 7;
    int s = 0, b = 0;
    if (!decode_item(cu, nseq, blockIdx.x >> 3, s, b)) return;
    s = __builtin_amdgcn_readfirstlane(s); b = __builtin_amdgcn_readfirstlane(b);
    const int start = cu[s], L = cu[s + 1] - start;
    const int NKT = (L + KVBLK - 1) / KVBLK;
    const int NT = min(2 * b + 2, NKT);
    const int grp = wid >> 2;
    const int head = 2 * g + grp;
    const int qrow0 = b * QB + (wid & 3) * 32;
    const int qrow = qrow0 + r32;

    char* K_lds = lds; char* V_lds = lds + 2 * SHM_T;
    bf16x8 qr[8];
    {
        const int qc = min(qrow, L - 1);
        const short* qp = Qg + ((size_t)(start + qc) * HQ + head) * D + hi * 8;
#pragma unroll
        for (int d0 = 0; d0 < 8; ++d0) qr[d0] = *reinterpret_cast<const bf16x8*>(qp + d0 * 16);
    }
    const int st = tid & 255, sr = st >> 4, c8 = st & 15;
    const short* src = grp ? Vg : Kg;
    const __amdgpu_buffer_rsrc_t rs = __builtin_amdgcn_make_buffer_rsrc((void*)(src + ((size_t)start * HKV + g) * D), (short)0, (L * HKV - g) * D * 2, 0x00020000);
    const int voff = (sr * KSTR + c8 * 8) * 2;
    int wo[4];
#pragma unroll
    for (int i = 0; i < 4; ++i) {
        const int row = sr + 16 * i;
        wo[i] = grp ? (2 * SHM_T + v_st(row, c8 * 8)) : (row * 256 + ((c8 ^ (row & 7)) << 4));
    }
    bf16x8 stg[4];
#define SLOAD(t) do { _Pragma("unroll") for (int i_ = 0; i_ < 4; ++i_) stg[i_] = __builtin_bit_cast(bf16x8, __builtin_amdgcn_raw_buffer_load_b128(rs, voff, ((t) * KVBLK + 16 * i_) * KSTR * 2, 0)); } while (0)
#define SWRITE(t) do { _Pragma("unroll") for (int i_ = 0; i_ < 4; ++i_) *(bf16x8*)(lds + wo[i_] + ((t) & 1) * SHM_T) = stg[i_]; } while (0)
    int kad[4];
#pragma unroll
    for (int d0 = 0; d0 < 4; ++d0) kad[d0] = r32 * 256 + (((2 * d0 + hi) ^ (r32 & 7)) << 4);
    const char* vrd0 = V_lds + v_rd_base(lane);

    float m_reg = -1e30f, l_reg = 0.f;
    f32x16 o[4] = {};
    f32x16 p0, p1; bf16x8 pa[4];
    const int qm = qrow - 4 * hi;

    SLOAD(0);
    if (grp == 0) { SWRITE(0); if (NT > 1) SLOAD(1); }
    __syncthreads();
    if (grp == 1) { SWRITE(0); if (NT > 1) SLOAD(1); __syncthreads(); }
    __builtin_amdgcn_s_setprio(1);
    qk_phase(p0, p1, K_lds, kad, qr);
    __builtin_amdgcn_s_setprio(0);
    __syncthreads();
    for (int t = 0; t < NT; ++t) {
        if (t * KVBLK + KVBLK - 1 > qrow0) {
            const int dq = qm - t * KVBLK;
            const float NEG = -__builtin_inff();
#pragma unroll
            for (int r = 0; r < 16; ++r) {
                const int c = (r & 3) + 8 * (r >> 2);
                if (c > dq) p0[r] = NEG;
                if (c + 32 > dq) p1[r] = NEG;
            }
        }
        float pmax = fmaxf(p0[0], p1[0]);
#pragma unroll
        for (int r = 1; r < 16; ++r) pmax = fmaxf(pmax, fmaxf(p0[r], p1[r]));
        { auto rr = __builtin_amdgcn_permlane32_swap(__float_as_uint(pmax), __float_as_uint(pmax), false, false);
          pmax = fmaxf(__uint_as_float(rr[0]), __uint_as_float(rr[1])); }
        const float mn = fmaxf(m_reg, pmax);
        const float alpha = __builtin_amdgcn_exp2f((m_reg - mn) * CEXP);
        m_reg = mn;
        const float mnL = -mn * CEXP;
        float ps = 0.f;
#pragma unroll
        for (int r = 0; r < 16; ++r) { p0[r] = __builtin_amdgcn_exp2f(fmaf(p0[r], CEXP, mnL)); ps += p0[r]; }
#pragma unroll
        for (int r = 0; r < 16; ++r) { p1[r] = __builtin_amdgcn_exp2f(fmaf(p1[r], CEXP, mnL)); ps += p1[r]; }
        l_reg = l_reg * alpha + ps;
#pragma unroll
        for (int i = 0; i < 2; ++i) {
            u32x4 w0 = {cvtpk(p0[8 * i + 0], p0[8 * i + 1]), cvtpk(p0[8 * i + 2], p0[8 * i + 3]), cvtpk(p0[8 * i + 4], p0[8 * i + 5]), cvtpk(p0[8 * i + 6], p0[8 * i + 7])};
            u32x4 w1 = {cvtpk(p1[8 * i + 0], p1[8 * i + 1]), cvtpk(p1[8 * i + 2], p1[8 * i + 3]), cvtpk(p1[8 * i + 4], p1[8 * i + 5]), cvtpk(p1[8 * i + 6], p1[8 * i + 7])};
            pa[i] = __builtin_bit_cast(bf16x8, w0); pa[2 + i] = __builtin_bit_cast(bf16x8, w1);
        }
        if (__any(alpha < 1.f)) {
#pragma unroll
            for (int d_ = 0; d_ < 4; ++d_)
#pragma unroll
                for (int r = 0; r < 16; ++r) o[d_][r] *= alpha;
        }
        if (t + 1 < NT) { SWRITE(t + 1); if (t + 2 < NT) SLOAD(t + 2); }
        __syncthreads();
        __builtin_amdgcn_s_setprio(1);
        pv_phase(o, vrd0 + (t & 1) * SHM_T, pa);
        if (t + 1 < NT) qk_phase(p0, p1, K_lds + ((t + 1) & 1) * SHM_T, kad, qr);
        __builtin_amdgcn_s_setprio(0);
        if (grp == 0 || t + 1 < NT) __syncthreads();
    }
    int lane2; asm volatile("v_mbcnt_lo_u32_b32 %0, -1, 0\n\tv_mbcnt_hi_u32_b32 %0, -1, %0" : "=v"(lane2));
    const float lt = l_reg + __shfl_xor(l_reg, 32);
    const float inv = 1.0f / lt;
    char* ost = lds + LDS_KV + wid * (32 * OROW);
#pragma unroll
    for (int d0 = 0; d0 < 4; ++d0)
#pragma unroll
        for (int a = 0; a < 4; ++a) {
            uint2 w;
            w.x = cvtpk(o[d0][4 * a + 0] * inv, o[d0][4 * a + 1] * inv);
            w.y = cvtpk(o[d0][4 * a + 2] * inv, o[d0][4 * a + 3] * inv);
            *(uint2*)(ost + (lane2 & 31) * OROW + (32 * d0 + 8 * a + 4 * (lane2 >> 5)) * 2) = w;
        }
    asm volatile("s_waitcnt lgkmcnt(0)" ::: "memory");
    __builtin_amdgcn_wave_barrier();
#pragma unroll
    for (int it = 0; it < 8; ++it) {
        const int rr = 4 * it + (lane2 >> 4), ch = lane2 & 15;
        const u32x4 w = *(const u32x4*)(ost + rr * OROW + ch * 16);
        const int orow = qrow0 + rr;
        if (orow < L) *(u32x4*)(Og + ((size_t)(start + orow) * HQ + head) * D + ch * 8) = w;
    }
}

extern "C" void kernel_launch(void* const* d_in, const int* in_sizes, int n_in,
                              void* d_out, int out_size, void* d_ws, size_t ws_size,
                              hipStream_t stream) {
    (void)n_in; (void)out_size; (void)d_ws; (void)ws_size;
    const short* q = (const short*)d_in[0];
    const short* k = (const short*)d_in[1];
    const short* v = (const short*)d_in[2];
    const int* cu = (const int*)d_in[3];
    short* out = (short*)d_out;
    const int T = in_sizes[0] / (HQ * D);
    const int nseq = in_sizes[3] - 1;
    const int maxpairs = (T + QB - 1) / QB + nseq;
    ragged_attn_kernel<<<dim3(8 * maxpairs), dim3(512), 0, stream>>>(q, k, v, cu, out, nseq);
}
```

```cpp
#include <hip/hip_runtime.h>
#include <hip/hip_bf16.h>
#include <cstdint>

typedef short bf16x8 __attribute__((ext_vector_type(8)));
typedef short s16x4 __attribute__((ext_vector_type(4)));
typedef float f32x16 __attribute__((ext_vector_type(16)));
typedef unsigned u32x4 __attribute__((ext_vector_type(4)));
typedef __attribute__((address_space(3))) s16x4* lds_s16x4_p;

constexpr int D = 128, HQ = 16, HKV = 8;
constexpr int KSTR = HKV * D;
constexpr int QB = 64;
constexpr int KVBLK = 64;
constexpr int SHM_T = KVBLK * D * 2;
constexpr int OROW = 272;
constexpr int LDS_KV = 4 * SHM_T;
constexpr int LDS_BYTES = LDS_KV;
constexpr float CEXP = 0.08838834764831845f * 1.4426950408889634f;
constexpr float THR = 8.f;
constexpr int BIGT = 18;

__device__ __forceinline__ unsigned cvtpk(float lo, float hi) {
    unsigned r; asm volatile("v_cvt_pk_bf16_f32 %0, %1, %2" : "=v"(r) : "v"(lo), "v"(hi)); return r;
}
__device__ __forceinline__ float max3f(float a, float b, float c) { float r; asm("v_max3_f32 %0, %1, %2, %3" : "=v"(r) : "v"(a), "v"(b), "v"(c)); return r; }
__device__ __forceinline__ float fadd_s(float a, float b) { float r; asm("v_add_f32_e32 %0, %1, %2" : "=v"(r) : "v"(a), "v"(b)); return r; }
__device__ __forceinline__ int v_st(int k, int c) { return ((k >> 3) * 4 + (c >> 5)) * 512 + ((k & 7) * 32 + (c & 31)) * 2; }
__device__ __forceinline__ int v_rd_base(int lane) { return ((lane & 3) << 3) | (((lane >> 2) & 3) << 6) | (((lane >> 4) & 1) << 5) | (((lane >> 5) & 1) << 8); }

__device__ __forceinline__ bool decode_item(const int* __restrict__ cu, int nseq, int rank, int& s_out, int& b_out) {
    const int lane = threadIdx.x & 63;
    if (nseq <= 64) {
        int nb = 0;
        if (lane < nseq) { const int L = cu[lane + 1] - cu[lane]; nb = (L + QB - 1) / QB; }
        int maxnb = nb;
#pragma unroll
        for (int o = 32; o >= 1; o >>= 1) maxnb = max(maxnb, __shfl_xor(maxnb, o));
        for (int bb = maxnb - 1; bb >= 0; --bb) {
            unsigned long long m = __ballot(nb > bb);
            const int c = __popcll(m);
            if (rank < c) {
                for (int i = 0; i < rank; ++i) m &= m - 1;
                s_out = __ffsll((long long)m) - 1; b_out = bb; return true;
            }
            rank -= c;
        }
        return false;
    } else {
        int maxnb = 0;
        for (int s = 0; s < nseq; ++s) { const int L = cu[s + 1] - cu[s]; maxnb = max(maxnb, (L + QB - 1) / QB); }
        for (int bb = maxnb - 1; bb >= 0; --bb)
            for (int s = 0; s < nseq; ++s) {
                const int L = cu[s + 1] - cu[s];
                if ((L + QB - 1) / QB > bb) { if (rank == 0) { s_out = s; b_out = bb; return true; } --rank; }
            }
        return false;
    }
}

#define MFMA(a, b, c) __builtin_amdgcn_mfma_f32_32x32x16_bf16(a, b, c, 0, 0, 0)
#define SB() __builtin_amdgcn_sched_barrier(0)
__device__ __forceinline__ s16x4 trrd(const char* p) { return __builtin_amdgcn_ds_read_tr16_b64_v4i16((lds_s16x4_p)p); }
__device__ __forceinline__ void loadV(bf16x8* f, const char* vr, int d0) {
#pragma unroll
    for (int s = 0; s < 4; ++s) {
        const s16x4 lo = trrd(vr + d0 * 512 + s * 4096), hi = trrd(vr + d0 * 512 + s * 4096 + 2048);
        f[s] = __builtin_shufflevector(lo, hi, 0, 1, 2, 3, 4, 5, 6, 7);
    }
}
__device__ __forceinline__ void loadK(bf16x8* f, const char* kb, int kad, int j) {
#pragma unroll
    for (int i = 0; i < 2; ++i) {
        const int d0 = 2 * j + i;
        const char* a = kb + (kad ^ (d0 << 5));
        f[2 * i] = *reinterpret_cast<const bf16x8*>(a);
        f[2 * i + 1] = *reinterpret_cast<const bf16x8*>(a + 32 * 256);
    }
}
__device__ __forceinline__ bf16x8 ldV1(const char* vr, int d0, int s) {
    const s16x4 lo = trrd(vr + d0 * 512 + s * 4096), hi = trrd(vr + d0 * 512 + s * 4096 + 2048);
    return __builtin_shufflevector(lo, hi, 0, 1, 2, 3, 4, 5, 6, 7);
}
__device__ __forceinline__ bf16x8 ldK1(const char* kb, int kad, int j, int i) {
    const int d0 = 2 * j + (i >> 1);
    return *reinterpret_cast<const bf16x8*>(kb + (kad ^ (d0 << 5)) + (i & 1) * 32 * 256);
}
__device__ __forceinline__ float dot2acc(unsigned pk, float acc) { typedef __bf16 bf2 __attribute__((ext_vector_type(2))); return __builtin_amdgcn_fdot2_f32_bf16(__builtin_bit_cast(bf2, pk), __builtin_bit_cast(bf2, 0x3f803f80u), acc, false); }
__device__ __forceinline__ void pvg(f32x16& o, const bf16x8* f, const bf16x8* pa) {
#pragma unroll
    for (int s = 0; s < 4; ++s) o = MFMA(f[s], pa[s], o);
}
__device__ __forceinline__ void qkg(f32x16& p0, f32x16& p1, const bf16x8* f, const bf16x8* qr, int j) {
    p0 = MFMA(f[0], qr[2 * j], p0); p1 = MFMA(f[1], qr[2 * j], p1);
    p0 = MFMA(f[2], qr[2 * j + 1], p0); p1 = MFMA(f[3], qr[2 * j + 1], p1);
}


__global__ __launch_bounds__(256, 2)
void ragged_attn_kernel(const short* __restrict__ Qg, const short* __restrict__ Kg, const short* __restrict__ Vg,
                        const int* __restrict__ cu, short* __restrict__ Og, int nseq) {
    __shared__ __attribute__((aligned(16))) char lds[LDS_BYTES];
    const int tid = threadIdx.x;
    const int wid = __builtin_amdgcn_readfirstlane(tid >> 6), lane = tid & 63, r32 = lane & 31, hi = lane >> 5;
    const int g = blockIdx.x & 7;
    int s = 0, b = 0, start = 0, L = 0;
    {
        const int rank = blockIdx.x >> 3;
        bool fast = nseq < 64;
        int cv = 0, nb = 0;
        if (fast) {
            cv = (lane <= nseq) ? cu[lane] : 0;
            const int nx = __shfl_down(cv, 1);
            nb = (lane < nseq) ? (nx - cv + QB - 1) / QB : 0;
            fast = !__any(nb > 63);
        }
        if (fast) {
            int C = 0;
            for (int i = 0; i < nseq; ++i) C += max(0, __builtin_amdgcn_readlane(nb, i) - lane);
            const unsigned long long mk = __ballot(C > rank);
            if (mk == 0) return;
            b = 63 - __builtin_clzll(mk);
            int r = rank - (b < 63 ? __builtin_amdgcn_readlane(C, b + 1) : 0);
            unsigned long long m = __ballot(nb > b);
            for (; r > 0; --r) m &= m - 1;
            s = __ffsll((long long)m) - 1;
            start = __builtin_amdgcn_readlane(cv, s); L = __builtin_amdgcn_readlane(cv, s + 1) - start;
        } else {
            if (!decode_item(cu, nseq, rank, s, b)) return;
            s = __builtin_amdgcn_readfirstlane(s); b = __builtin_amdgcn_readfirstlane(b);
            start = cu[s]; L = cu[s + 1] - start;
        }
    }
    b = __builtin_amdgcn_readfirstlane(b); start = __builtin_amdgcn_readfirstlane(start); L = __builtin_amdgcn_readfirstlane(L);
    const int NT = b + 1;
    const bool big = NT > BIGT;
    const int head = 2 * g + (wid >> 1);
    const int qrow0 = b * QB + (wid & 1) * 32;
    const int qrow = qrow0 + r32;

    char* K_lds = lds; char* V_lds = lds + 2 * SHM_T;
    const int sr = tid >> 4, c8 = tid & 15;
    const __amdgpu_buffer_rsrc_t rsK = __builtin_amdgcn_make_buffer_rsrc((void*)(Kg + ((size_t)start * HKV + g) * D), (short)0, (L * HKV - g) * D * 2, 0x00020000);
    const __amdgpu_buffer_rsrc_t rsV = __builtin_amdgcn_make_buffer_rsrc((void*)(Vg + ((size_t)start * HKV + g) * D), (short)0, (L * HKV - g) * D * 2, 0x00020000);
    const int voff = (sr * KSTR + c8 * 8) * 2;
    const int woK = sr * 256 + ((c8 ^ (sr & 15)) << 4);
    const int woV = 2 * SHM_T + v_st(sr, c8 * 8);
    bf16x8 stK[4], stV[4];
#define SLOADK(t) do { _Pragma("unroll") for (int i_ = 0; i_ < 4; ++i_) stK[i_] = __builtin_bit_cast(bf16x8, __builtin_amdgcn_raw_buffer_load_b128(rsK, voff, ((t) * KVBLK + 16 * i_) * KSTR * 2, 0)); } while (0)
#define SLOADV(t) do { _Pragma("unroll") for (int i_ = 0; i_ < 4; ++i_) stV[i_] = __builtin_bit_cast(bf16x8, __builtin_amdgcn_raw_buffer_load_b128(rsV, voff, ((t) * KVBLK + 16 * i_) * KSTR * 2, 0)); } while (0)
#define SWRITEK(t) do { _Pragma("unroll") for (int i_ = 0; i_ < 4; ++i_) *(bf16x8*)(lds + woK + i_ * 4096 + ((t) & 1) * SHM_T) = stK[i_]; } while (0)
#define SWRITEV(t) do { _Pragma("unroll") for (int i_ = 0; i_ < 4; ++i_) *(bf16x8*)(lds + woV + i_ * 4096 + ((t) & 1) * SHM_T) = stV[i_]; } while (0)
    SLOADK(0); SLOADV(0);
    bf16x8 qr[8];
    {
        const int qc = min(qrow, L - 1);
        const short* qp = Qg + ((size_t)(start + qc) * HQ + head) * D + hi * 8;
#pragma unroll
        for (int d0 = 0; d0 < 8; ++d0) qr[d0] = *reinterpret_cast<const bf16x8*>(qp + d0 * 16);
#pragma unroll
        for (int d0 = 0; d0 < 8; ++d0) {
            u32x4 w = __builtin_bit_cast(u32x4, qr[d0]), r;
#pragma unroll
            for (int e = 0; e < 4; ++e) r[e] = cvtpk(__uint_as_float(w[e] << 16) * CEXP, __uint_as_float(w[e] & 0xffff0000u) * CEXP);
            qr[d0] = __builtin_bit_cast(bf16x8, r);
        }
    }
    const int kad = r32 * 256 + ((hi ^ (r32 & 15)) << 4);
    const char* vrd0 = V_lds + v_rd_base(lane);

    float mhat = 0.f, l_reg = 0.f;
    float thr = -__builtin_inff(), dfloor = -__builtin_inff();
    asm volatile("" : "+v"(thr), "+v"(dfloor));
    f32x16 negm = {};
    f32x16 o[4] = {};
    f32x16 p0, p1; bf16x8 pa[4], fa[4], fb[4];
    const int qm = qrow - 4 * hi;

    SWRITEK(0); SWRITEV(0);
    SLOADK(1); SLOADV(1);
    __syncthreads();
    if (big) __builtin_amdgcn_s_setprio(3); else __builtin_amdgcn_s_setprio(1);
    {
        const char* kb = K_lds;
#pragma unroll
        for (int i = 0; i < 4; ++i) fa[i] = ldK1(kb, kad, 0, i);
        SB();
        p0 = negm; p1 = negm;
#pragma unroll
        for (int j = 0; j < 4; ++j) {
            bf16x8* fc = (j & 1) ? fb : fa; bf16x8* fn = (j & 1) ? fa : fb;
#pragma unroll
            for (int i = 0; i < 4; ++i) {
                if (i & 1) p1 = MFMA(fc[i], qr[2 * j + (i >> 1)], p1); else p0 = MFMA(fc[i], qr[2 * j + (i >> 1)], p0);
                SB();
                if (j < 3) fn[i] = ldK1(kb, kad, j + 1, i);
                if (j == 2) *(bf16x8*)(lds + woK + i * 4096 + SHM_T) = stK[i];
                if (j == 3) stK[i] = __builtin_bit_cast(bf16x8, __builtin_amdgcn_raw_buffer_load_b128(rsK, voff, (2 * KVBLK + 16 * i) * KSTR * 2, 0));
                SB();
            }
        }
    }
    if (big) __builtin_amdgcn_s_setprio(2); else __builtin_amdgcn_s_setprio(0);
    for (int t = 0; t < NT; ++t) {
        if (t == NT - 1) {
            const int dq = qm - t * KVBLK;
            const float NEG = -__builtin_inff();
#pragma unroll
            for (int r = 0; r < 16; ++r) {
                const int c = (r & 3) + 8 * (r >> 2);
                if (c > dq) p0[r] = NEG;
                if (c + 32 > dq) p1[r] = NEG;
            }
        }
        float pm0 = max3f(p0[0], p0[1], p0[2]), pm1 = max3f(p0[4], p0[5], p0[6]), pm2 = max3f(p1[0], p1[1], p1[2]), pm3 = max3f(p1[4], p1[5], p1[6]);
        pm0 = max3f(pm0, p0[3], p0[8]); pm1 = max3f(pm1, p0[7], p0[12]); pm2 = max3f(pm2, p1[3], p1[8]); pm3 = max3f(pm3, p1[7], p1[12]);
        pm0 = max3f(pm0, p0[9], p0[10]); pm1 = max3f(pm1, p0[13], p0[14]); pm2 = max3f(pm2, p1[9], p1[10]); pm3 = max3f(pm3, p1[13], p1[14]);
        pm0 = max3f(pm0, p0[11], p0[15]); pm2 = max3f(pm2, p1[11], p1[15]);
        float pmax = max3f(pm0, pm1, fmaxf(pm2, pm3));
        { auto rr = __builtin_amdgcn_permlane32_swap(__float_as_uint(pmax), __float_as_uint(pmax), false, false);
          pmax = fmaxf(__uint_as_float(rr[0]), __uint_as_float(rr[1])); }
        if (__builtin_expect(__any(pmax > thr), 0)) {
            const float delta = fmaxf(pmax, dfloor);
            const float f = __builtin_amdgcn_exp2f(-fmaxf(delta, 0.f));
            mhat += delta;
            const float nm = -mhat;
#pragma unroll
            for (int r = 0; r < 16; ++r) {
                asm volatile("v_sub_f32 %0, %0, %1" : "+v"(p0[r]) : "v"(delta));
                asm volatile("v_sub_f32 %0, %0, %1" : "+v"(p1[r]) : "v"(delta));
                asm volatile("v_mov_b32 %0, %1" : "=v"(negm[r]) : "v"(nm));
            }
            l_reg *= f;
#pragma unroll
            for (int d_ = 0; d_ < 4; ++d_)
#pragma unroll
                for (int r = 0; r < 16; ++r) asm volatile("v_mul_f32 %0, %0, %1" : "+v"(o[d_][r]) : "v"(f));
            thr = THR; dfloor = 0.f;
        }
        {
            unsigned pk[16];
#define EXP4(P, B_) asm volatile("v_exp_f32 %0, %0\n\tv_exp_f32 %1, %1\n\tv_exp_f32 %2, %2\n\tv_exp_f32 %3, %3" : "+v"(P[B_]), "+v"(P[B_+1]), "+v"(P[B_+2]), "+v"(P[B_+3]))
#define CVT2(P, B_, K_) asm volatile("v_cvt_pk_bf16_f32 %0, %2, %3\n\tv_cvt_pk_bf16_f32 %1, %4, %5" : "=&v"(pk[K_]), "=&v"(pk[K_+1]) : "v"(P[B_]), "v"(P[B_+1]), "v"(P[B_+2]), "v"(P[B_+3]))
            float s0 = l_reg, s1 = 0.f;
#define DOT2(K_) do { s0 = dot2acc(pk[K_], s0); s1 = dot2acc(pk[K_+1], s1); } while (0)
            EXP4(p0, 0);
            EXP4(p0, 4);  CVT2(p0, 0, 0);
            EXP4(p0, 8);  CVT2(p0, 4, 2);  DOT2(0);
            EXP4(p0, 12); CVT2(p0, 8, 4);  DOT2(2);
            EXP4(p1, 0);  CVT2(p0, 12, 6); DOT2(4);
            EXP4(p1, 4);  CVT2(p1, 0, 8);  DOT2(6);
            EXP4(p1, 8);  CVT2(p1, 4, 10); DOT2(8);
            EXP4(p1, 12); CVT2(p1, 8, 12); DOT2(10);
            asm volatile("s_nop 1");
            CVT2(p1, 12, 14); DOT2(12); DOT2(14);
            l_reg = s0 + s1;
#pragma unroll
            for (int i = 0; i < 4; ++i) { u32x4 w = {pk[4 * i], pk[4 * i + 1], pk[4 * i + 2], pk[4 * i + 3]}; pa[i] = __builtin_bit_cast(bf16x8, w); }
        }
        __syncthreads();
        if (big) __builtin_amdgcn_s_setprio(3); else __builtin_amdgcn_s_setprio(1);
        {
            const char* vr = vrd0 + (t & 1) * SHM_T;
            const char* kb = K_lds + ((t + 1) & 1) * SHM_T;
#pragma unroll
            for (int i = 0; i < 4; ++i) fa[i] = ldV1(vr, 0, i);
            SB();
#pragma unroll
            for (int j = 0; j < 4; ++j) {
                bf16x8* fc = (j & 1) ? fb : fa; bf16x8* fn = (j & 1) ? fa : fb;
#pragma unroll
                for (int i = 0; i < 4; ++i) {
                    o[j] = MFMA(fc[i], pa[i], o[j]);
                    SB();
                    if (j < 3) fn[i] = ldV1(vr, j + 1, i); else fn[i] = ldK1(kb, kad, 0, i);
                    SB();
                }
            }
            if (t + 1 < NT) {
                p0 = negm; p1 = negm;
                const int vb = ((t + 1) & 1) * SHM_T, kbo = (t & 1) * SHM_T;
#pragma unroll
                for (int j = 0; j < 4; ++j) {
                    bf16x8* fc = (j & 1) ? fb : fa; bf16x8* fn = (j & 1) ? fa : fb;
#pragma unroll
                    for (int i = 0; i < 4; ++i) {
                        if (i & 1) p1 = MFMA(fc[i], qr[2 * j + (i >> 1)], p1); else p0 = MFMA(fc[i], qr[2 * j + (i >> 1)], p0);
                        SB();
                        if (j < 3) fn[i] = ldK1(kb, kad, j + 1, i);
                        if (j == 0) *(bf16x8*)(lds + woV + i * 4096 + vb) = stV[i];
                        if (j == 1) stV[i] = __builtin_bit_cast(bf16x8, __builtin_amdgcn_raw_buffer_load_b128(rsV, voff, ((t + 2) * KVBLK + 16 * i) * KSTR * 2, 0));
                        if (j == 2) *(bf16x8*)(lds + woK + i * 4096 + kbo) = stK[i];
                        if (j == 3) stK[i] = __builtin_bit_cast(bf16x8, __builtin_amdgcn_raw_buffer_load_b128(rsK, voff, ((t + 3) * KVBLK + 16 * i) * KSTR * 2, 0));
                        SB();
                    }
                }
            }
        }
        if (big) __builtin_amdgcn_s_setprio(2); else __builtin_amdgcn_s_setprio(0);
    }
    __syncthreads();
    int lane2; asm volatile("v_mbcnt_lo_u32_b32 %0, -1, 0\n\tv_mbcnt_hi_u32_b32 %0, -1, %0" : "=v"(lane2));
    const float lt = l_reg + __shfl_xor(l_reg, 32);
    const float inv = 1.0f / lt;
    char* ost = lds + wid * (32 * OROW);
#pragma unroll
    for (int d0 = 0; d0 < 4; ++d0)
#pragma unroll
        for (int a = 0; a < 4; ++a) {
            uint2 w;
            w.x = cvtpk(o[d0][4 * a + 0] * inv, o[d0][4 * a + 1] * inv);
            w.y = cvtpk(o[d0][4 * a + 2] * inv, o[d0][4 * a + 3] * inv);
            *(uint2*)(ost + (lane2 & 31) * OROW + (32 * d0 + 8 * a + 4 * (lane2 >> 5)) * 2) = w;
        }
    asm volatile("s_waitcnt lgkmcnt(0)" ::: "memory");
    __builtin_amdgcn_wave_barrier();
#pragma unroll
    for (int it = 0; it < 8; ++it) {
        const int rr = 4 * it + (lane2 >> 4), ch = lane2 & 15;
        const u32x4 w = *(const u32x4*)(ost + rr * OROW + ch * 16);
        const int orow = qrow0 + rr;
        if (orow < L) *(u32x4*)(Og + ((size_t)(start + orow) * HQ + head) * D + ch * 8) = w;
    }
}

extern "C" void kernel_launch(void* const* d_in, const int* in_sizes, int n_in,
                              void* d_out, int out_size, void* d_ws, size_t ws_size,
                              hipStream_t stream) {
    (void)n_in; (void)out_size; (void)d_ws; (void)ws_size;
    const short* q = (const short*)d_in[0];
    const short* k = (const short*)d_in[1];
    const short* v = (const short*)d_in[2];
    const int* cu = (const int*)d_in[3];
    short* out = (short*)d_out;
    const int T = in_sizes[0] / (HQ * D);
    const int nseq = in_sizes[3] - 1;
    const int maxpairs = (T + QB - 1) / QB + nseq;
    ragged_attn_kernel<<<dim3(8 * maxpairs), dim3(256), 0, stream>>>(q, k, v, cu, out, nseq);
}
```
